```python
import torch

def _softmax(x, axis=-1):
    return torch.softmax(x, dim=axis)
def _gelu(x):
    return torch.nn.functional.gelu(x, approximate="tanh")
def _silu(x):
    return torch.nn.functional.silu(x)
def _sigmoid(x):
    return torch.sigmoid(x)
def _layernorm(x, axis=-1, eps=1e-5):
    m = x.mean(dim=axis, keepdim=True)
    v = x.var(dim=axis, keepdim=True, unbiased=False)
    return (x - m) / torch.sqrt(v + eps)
def _scatter_add(tgt, idx, src, axis=0):
    return tgt.clone().index_add_(axis, idx, src)
def _segment_sum(src, seg, n):
    out = torch.zeros((n,) + src.shape[1:], dtype=src.dtype)
    return out.index_add_(0, seg, src)
def _pad(x, pw):
    flat = tuple(v for pair in reversed(pw) for v in pair)
    return torch.nn.functional.pad(x, flat)

kernel_name = 'concat_heads__sliding_block_cached__compressed_kv_cla__decode'

def setup_inputs():

    _w = (lambda a: (lambda t: t.to(torch.bfloat16) if t.is_floating_point() else t)(torch.as_tensor(a)))
    def _A_setup():
        B, Sq, D = 4, 1, 512
        Hq, Hkv, Sk, Dh = 32, 8, 8192, 128
        assert Hq % Hkv == 0, (Hq, Hkv)
        return {
            "x": _w((lambda *s: torch.randn(*s, dtype=torch.bfloat16) * 1.6)(B, Sq, D)),
            "k_cache": _w((lambda *s: torch.randn(*s, dtype=torch.bfloat16) * 1.6)(B, Hkv, Sk, Dh)),
            "v_cache": _w((lambda *s: torch.randn(*s, dtype=torch.bfloat16) * 0.05)(B, Hkv, Sk, Dh)),
            "wq": _w((lambda *s: torch.randn(*s, dtype=torch.bfloat16) * 1.6)(D, Hq * Dh) * (D ** -0.5)),
            "wo": _w((lambda *s: torch.randn(*s, dtype=torch.bfloat16) * 0.05)(Hq * Dh, D) * ((Hq * Dh) ** -0.5)),
        }
    _A_in = _A_setup()

    def _B_setup():
        B, H, Sq, Sk, D, M = 4, 32, 1, 8192, 128, 512
        Dc = 4 * D
        return {
            "x": _w((lambda *s: torch.randn(*s, dtype=torch.bfloat16) * 3.2)(B, Sq, M)),
            "c_kv": _w((lambda *s: torch.randn(*s, dtype=torch.bfloat16) * 0.1)(B, Sk, Dc)),
            "w_q": _w((lambda *s: torch.randn(*s, dtype=torch.bfloat16) * 6.4)(M, H, D) * (M ** -0.5)),
            "w_uk": _w((lambda *s: torch.randn(*s, dtype=torch.bfloat16) * 6.4)(Dc, H, D) * (Dc ** -0.5)),
            "w_uv": _w((lambda *s: torch.randn(*s, dtype=torch.bfloat16) * 0.1)(Dc, H, D) * (Dc ** -0.5)),
            "w_o": _w((lambda *s: torch.randn(*s, dtype=torch.bfloat16) * 0.1)(H, D, M) * ((H * D) ** -0.5)),
        }
    _B_in = _B_setup()

    return {
        "A_x": _A_in["x"],
        "A_k_cache": _A_in["k_cache"],
        "A_v_cache": _A_in["v_cache"],
        "A_wq": _A_in["wq"],
        "A_wo": _A_in["wo"],
        "B_x": _B_in["x"],
        "B_c_kv": _B_in["c_kv"],
        "B_w_q": _B_in["w_q"],
        "B_w_uk": _B_in["w_uk"],
        "B_w_uv": _B_in["w_uv"],
        "B_w_o": _B_in["w_o"],
    }

def reference(A_x, A_k_cache, A_v_cache, A_wq, A_wo, B_x, B_c_kv, B_w_q, B_w_uk, B_w_uv, B_w_o):
    def _A_ref(x, k_cache, v_cache, wq, wo):
        B, Sq, D = x.shape
        Hq, Dh = 32, 128
        Hkv, Sk = k_cache.shape[1], k_cache.shape[2]
        W = min(4096, max(256, Sk // 4))
        rep = Hq // Hkv
        q = (lambda x, *d: x.permute(*d))((x @ wq).reshape(B, Sq, Hq, Dh), 0, 2, 1, 3)
        k = (lambda x, n, axis: torch.repeat_interleave(x, n, dim=axis))(k_cache[:, :, Sk - W:, :], rep, axis=1)
        v = (lambda x, n, axis: torch.repeat_interleave(x, n, dim=axis))(v_cache[:, :, Sk - W:, :], rep, axis=1)
        scores = torch.einsum("bhqd,bhkd->bhqk", q, k) * (Dh ** -0.5)
        p = _softmax(scores, axis=-1)
        o = torch.einsum("bhqk,bhkd->bhqd", p, v)
        return (lambda x, *d: x.permute(*d))(o, 0, 2, 1, 3).reshape(B, Sq, Hq * Dh) @ wo

    def _B_ref(x, c_kv, w_q, w_uk, w_uv, w_o):
        B, Sq, M = x.shape[0], x.shape[1], x.shape[2]
        Sk = c_kv.shape[1]
        H, D = w_q.shape[1], w_q.shape[2]
        q = torch.einsum("bqm,mhd->bhqd", x, w_q)
        k = torch.einsum("bkc,chd->bhkd", c_kv, w_uk)
        v = torch.einsum("bkc,chd->bhkd", c_kv, w_uv)
        scores = torch.einsum("bhqd,bhkd->bhqk", q, k) * (D ** -0.5)
        off = Sk - Sq
        qi = torch.arange(Sq)[:, None]
        kj = torch.arange(Sk)[None, :]
        cmask = kj <= (qi + off)
        scores = torch.where(cmask[None, None], scores, -1e9)
        p = _softmax(scores, axis=-1)
        ctx = torch.einsum("bhqk,bhkd->bhqd", p, v)
        return torch.einsum("bhqd,hdm->bqm", ctx, w_o)

    a = _A_ref(A_x, A_k_cache, A_v_cache, A_wq, A_wo)
    b = _B_ref(B_x, B_c_kv, B_w_q, B_w_uk, B_w_uv, B_w_o)
    return torch.concatenate([a, b], axis=-1)
```

```cpp
#include <hip/hip_runtime.h>
#include <hip/hip_bf16.h>
#include <stdint.h>

typedef unsigned short u16;
using bf16x8 = __attribute__((ext_vector_type(8))) short;
using s16x4  = __attribute__((ext_vector_type(4))) short;
using f32x16 = __attribute__((ext_vector_type(16))) float;
using f32x4  = __attribute__((ext_vector_type(4))) float;
using u32x4  = __attribute__((ext_vector_type(4))) unsigned;
#define DI __device__ __forceinline__
#define MFMA32(a, b, c) __builtin_amdgcn_mfma_f32_32x32x16_bf16((a), (b), (c), 0, 0, 0)

constexpr int NB = 4;
constexpr int DM = 512;
constexpr int HQ = 32, HKV = 8, SK = 8192, DH = 128, WIN = 2048;
constexpr int DC = 512;
constexpr int NSPLIT_B = 32;
constexpr int KEYS_B = SK / NSPLIT_B;
constexpr int NSPLIT_A = 16;
constexpr int KEYS_A = WIN / NSPLIT_A;

constexpr size_t OFF_QA   = 0;
constexpr size_t OFF_QABS = OFF_QA + 4 * 4096 * 2;
constexpr size_t OFF_ACCB = OFF_QABS + 4 * 32 * 512 * 2;
constexpr size_t OFF_MLB  = OFF_ACCB + (size_t)128 * 32 * 512 * 4;
constexpr size_t OFF_ACCA = OFF_MLB + 128 * 32 * 2 * 4;
constexpr size_t OFF_MLA  = OFF_ACCA + (size_t)512 * 4 * 128 * 4;
constexpr size_t OFF_OP   = OFF_MLA + 512 * 4 * 2 * 4;

DI float bf2f(u16 x) { return __uint_as_float(((unsigned)x) << 16); }
DI float bfl(unsigned x) { return __uint_as_float(x << 16); }
DI float bfh(unsigned x) { return __uint_as_float(x & 0xffff0000u); }
DI u16 f2bf(float x) { unsigned u = __float_as_uint(x); u += 0x7fffu + ((u >> 16) & 1u); return (u16)(u >> 16); }
DI float rbf(float x) { return bf2f(f2bf(x)); }

DI unsigned xr_b(unsigned row) { return ((row & 3) << 2) | ((row >> 2) & 3); }
DI unsigned off_b(unsigned row, unsigned ch) { return 256u * row + 16u * (ch ^ xr_b(row)); }

DI void glds16(const void* gsrc, unsigned lds_dst) {
  unsigned keep;
  asm volatile("s_mov_b32 %0, m0\n\ts_mov_b32 m0, %2\n\ts_nop 0\n\tglobal_load_lds_dwordx4 %1, off\n\ts_mov_b32 m0, %0"
               : "=&s"(keep) : "v"(gsrc), "s"(lds_dst) : "memory");
}
#define WAIT_VM(n) asm volatile("s_waitcnt vmcnt(" #n ")" ::: "memory")

DI bf16x8 pack_step(const f32x16& x, int s) {
  u32x4 p;
  asm volatile("s_nop 1\n\tv_cvt_pk_bf16_f32 %0, %4, %5\n\tv_cvt_pk_bf16_f32 %1, %6, %7\n\tv_cvt_pk_bf16_f32 %2, %8, %9\n\tv_cvt_pk_bf16_f32 %3, %10, %11\n\ts_nop 1"
               : "=&v"(p[0]), "=&v"(p[1]), "=&v"(p[2]), "=&v"(p[3])
               : "v"(x[8 * s]), "v"(x[8 * s + 1]), "v"(x[8 * s + 2]), "v"(x[8 * s + 3]), "v"(x[8 * s + 4]), "v"(x[8 * s + 5]), "v"(x[8 * s + 6]), "v"(x[8 * s + 7]));
  return __builtin_bit_cast(bf16x8, p);
}

DI void tr_read4(unsigned a0, unsigned a1, unsigned a2, unsigned a3, bf16x8& f0, bf16x8& f1) {
  s16x4 r0, r1, r2, r3;
  asm volatile("ds_read_b64_tr_b16 %0, %4\n\tds_read_b64_tr_b16 %1, %5\n\tds_read_b64_tr_b16 %2, %6\n\tds_read_b64_tr_b16 %3, %7\n\ts_waitcnt lgkmcnt(0)"
               : "=&v"(r0), "=&v"(r1), "=&v"(r2), "=&v"(r3) : "v"(a0), "v"(a1), "v"(a2), "v"(a3) : "memory");
  f0 = __builtin_shufflevector(r0, r1, 0, 1, 2, 3, 4, 5, 6, 7);
  f1 = __builtin_shufflevector(r2, r3, 0, 1, 2, 3, 4, 5, 6, 7);
}

__global__ __launch_bounds__(256) void k1_qproj(const u16* __restrict__ xA, const u16* __restrict__ wq,
                                                 const u16* __restrict__ xB, const u16* __restrict__ wqB,
                                                 const u16* __restrict__ wuk, u16* __restrict__ qA, u16* __restrict__ qabs) {
  __shared__ float xs[4][512];
  __shared__ float red[4][4][128];
  __shared__ float qb[4][128];
  const int tid = threadIdx.x, lane = tid & 63, w = tid >> 6;
  const bool partA = blockIdx.x < 64;
  const u16* x = partA ? xA : xB;
  for (int i = tid; i < 2048; i += 256) xs[i >> 9][i & 511] = bf2f(x[i]);
  __syncthreads();
  if (partA) {
    const int col0 = blockIdx.x * 64;
    const int rg = lane >> 3, cg = lane & 7;
    float acc[4][8];
#pragma unroll
    for (int b = 0; b < 4; ++b)
#pragma unroll
      for (int j = 0; j < 8; ++j) acc[b][j] = 0.f;
#pragma unroll
    for (int i = 0; i < 16; ++i) {
      const int row = w * 128 + i * 8 + rg;
      const u32x4 v = *(const u32x4*)(wq + (size_t)row * 4096 + col0 + cg * 8);
#pragma unroll
      for (int b = 0; b < 4; ++b) {
        const float xv = xs[b][row];
#pragma unroll
        for (int j = 0; j < 4; ++j) { acc[b][2 * j] += xv * bfl(v[j]); acc[b][2 * j + 1] += xv * bfh(v[j]); }
      }
    }
#pragma unroll
    for (int b = 0; b < 4; ++b)
#pragma unroll
      for (int j = 0; j < 8; ++j) {
        float s = acc[b][j];
        s += __shfl_xor(s, 8); s += __shfl_xor(s, 16); s += __shfl_xor(s, 32);
        if (rg == 0) red[w][b][cg * 8 + j] = s;
      }
    __syncthreads();
    {
      const int b = tid >> 6, c = tid & 63;
      const float s = red[0][b][c] + red[1][b][c] + red[2][b][c] + red[3][b][c];
      qA[b * 4096 + col0 + c] = f2bf(s);
    }
  } else {
    const int id = blockIdx.x - 64, h = id >> 3, cs = id & 7;
    const int rg = tid >> 4, cg = tid & 15;
    float acc[4][8];
#pragma unroll
    for (int b = 0; b < 4; ++b)
#pragma unroll
      for (int j = 0; j < 8; ++j) acc[b][j] = 0.f;
#pragma unroll 8
    for (int i = 0; i < 32; ++i) {
      const int row = i * 16 + rg;
      const u32x4 v = *(const u32x4*)(wqB + (size_t)row * 4096 + h * 128 + cg * 8);
#pragma unroll
      for (int b = 0; b < 4; ++b) {
        const float xv = xs[b][row];
#pragma unroll
        for (int j = 0; j < 4; ++j) { acc[b][2 * j] += xv * bfl(v[j]); acc[b][2 * j + 1] += xv * bfh(v[j]); }
      }
    }
#pragma unroll
    for (int b = 0; b < 4; ++b)
#pragma unroll
      for (int j = 0; j < 8; ++j) {
        float s = acc[b][j];
        s += __shfl_xor(s, 16); s += __shfl_xor(s, 32);
        if (lane < 16) red[w][b][cg * 8 + j] = s;
      }
    __syncthreads();
    for (int i = tid; i < 512; i += 256) {
      const int b = i >> 7, d = i & 127;
      qb[b][d] = rbf(red[0][b][d] + red[1][b][d] + red[2][b][d] + red[3][b][d]);
    }
    __syncthreads();
#pragma unroll
    for (int i = 0; i < 4; ++i) {
      const int c = cs * 64 + i * 16 + rg;
      const u32x4 v = *(const u32x4*)(wuk + (size_t)c * 4096 + h * 128 + cg * 8);
#pragma unroll
      for (int b = 0; b < 4; ++b) {
        float s = 0.f;
#pragma unroll
        for (int j = 0; j < 4; ++j) { s += qb[b][cg * 8 + 2 * j] * bfl(v[j]); s += qb[b][cg * 8 + 2 * j + 1] * bfh(v[j]); }
        s += __shfl_xor(s, 1); s += __shfl_xor(s, 2); s += __shfl_xor(s, 4); s += __shfl_xor(s, 8);
        if (cg == 0) qabs[(b * 32 + h) * 512 + c] = f2bf(s);
      }
    }
  }
}

constexpr int RING_B = 3;
__global__ __launch_bounds__(256) void k2_attn(const u16* __restrict__ kc, const u16* __restrict__ vc,
                                                const u16* __restrict__ ckv, const u16* __restrict__ qA,
                                                const u16* __restrict__ qabs, float* __restrict__ accB,
                                                float* __restrict__ mlB, float* __restrict__ accA, float* __restrict__ mlA) {
  extern __shared__ __attribute__((aligned(16))) char smem[];
  const int tid = threadIdx.x, lane = tid & 63;
  const int w = __builtin_amdgcn_readfirstlane(tid >> 6);
  const int r = lane & 31, h = lane >> 5;
  const unsigned sbase = (unsigned)(size_t)smem;
  const float LOG2E = 1.4426950408889634f;
  const float scale = 0.08838834764831845f * LOG2E;

  const unsigned blk = (lane >> 4) & 1, q4 = (lane & 15) >> 2, p = lane & 3;
  unsigned trof[4][2][2];
#pragma unroll
  for (int ct = 0; ct < 4; ++ct)
#pragma unroll
    for (int ks = 0; ks < 2; ++ks)
#pragma unroll
      for (int t = 0; t < 2; ++t)
        trof[ct][ks][t] = off_b(16 * ks + 8 * t + 4 * h + q4, 4 * ct + 2 * blk + (p >> 1)) + 8 * (p & 1);
  unsigned rowof[8];
#pragma unroll
  for (int s = 0; s < 8; ++s) rowof[s] = off_b(r, 2 * s + h);

  f32x16 acc[4];
#pragma unroll
  for (int ct = 0; ct < 4; ++ct)
#pragma unroll
    for (int i = 0; i < 16; ++i) acc[ct][i] = 0.f;
  float m_run = -1e30f, l_run = 0.f;

  if (blockIdx.x < 128) {
    const int wg = blockIdx.x, b = wg >> 5, sp = wg & 31;
    const int key0 = sp * KEYS_B;
    const u16* src = ckv + ((size_t)b * SK + key0) * DC + w * 128;
    const unsigned ring = sbase + w * (RING_B * 8192);
    const unsigned sx = sbase + 4 * RING_B * 8192;
    unsigned goff[8];
#pragma unroll
    for (int i = 0; i < 8; ++i) {
      const unsigned n = lane + 64 * i, row = n >> 4, ch = (n & 15) ^ xr_b(row);
      goff[i] = row * DC + ch * 8;
    }
    auto issue = [&](int t) {
      const u16* tp = src + (size_t)t * 32 * DC;
      const unsigned dst = ring + (t % RING_B) * 8192;
#pragma unroll
      for (int i = 0; i < 8; ++i) glds16(tp + goff[i], dst + i * 1024);
    };
#pragma unroll
    for (int t = 0; t < RING_B; ++t) issue(t);
    bf16x8 qf[8];
#pragma unroll
    for (int s = 0; s < 8; ++s) qf[s] = *(const bf16x8*)(qabs + (b * 32 + r) * 512 + w * 128 + 16 * s + 8 * h);

#pragma unroll
    for (int t = 0; t < 8; ++t) {
      if (t == 0) { WAIT_VM(0); }
      else if (t <= 8 - RING_B) { WAIT_VM(16); }
      else if (t == 8 - RING_B + 1) { WAIT_VM(8); }
      else { WAIT_VM(0); }
      const unsigned tb = ring + (t % RING_B) * 8192;
      f32x16 s;
#pragma unroll
      for (int i = 0; i < 16; ++i) s[i] = 0.f;
#pragma unroll
      for (int ks = 0; ks < 8; ++ks) {
        const bf16x8 a = *(const bf16x8*)(smem + (tb - sbase) + rowof[ks]);
        s = MFMA32(a, qf[ks], s);
      }
      const unsigned sb = sx + (t & 1) * 16384;
#pragma unroll
      for (int g = 0; g < 4; ++g) {
        f32x4 v = {s[4 * g], s[4 * g + 1], s[4 * g + 2], s[4 * g + 3]};
        *(f32x4*)(smem + (sb - sbase) + w * 4096 + g * 1024 + lane * 16) = v;
      }
      asm volatile("s_waitcnt lgkmcnt(0)" ::: "memory");
      __builtin_amdgcn_s_barrier();
      asm volatile("" ::: "memory");
#pragma unroll
      for (int g = 0; g < 4; ++g) {
        f32x4 v0 = *(const f32x4*)(smem + (sb - sbase) + 0 * 4096 + g * 1024 + lane * 16);
        f32x4 v1 = *(const f32x4*)(smem + (sb - sbase) + 1 * 4096 + g * 1024 + lane * 16);
        f32x4 v2 = *(const f32x4*)(smem + (sb - sbase) + 2 * 4096 + g * 1024 + lane * 16);
        f32x4 v3 = *(const f32x4*)(smem + (sb - sbase) + 3 * 4096 + g * 1024 + lane * 16);
#pragma unroll
        for (int j = 0; j < 4; ++j) s[4 * g + j] = ((v0[j] + v1[j]) + (v2[j] + v3[j])) * scale;
      }
      float mx = s[0];
#pragma unroll
      for (int i = 1; i < 16; ++i) mx = fmaxf(mx, s[i]);
      mx = fmaxf(mx, __shfl_xor(mx, 32));
      const float m_new = fmaxf(m_run, mx);
      const float alpha = exp2f(m_run - m_new);
      m_run = m_new;
      float ps = 0.f;
#pragma unroll
      for (int i = 0; i < 16; ++i) { s[i] = exp2f(s[i] - m_new); ps += s[i]; }
      l_run = l_run * alpha + ps;
#pragma unroll
      for (int ct = 0; ct < 4; ++ct)
#pragma unroll
        for (int i = 0; i < 16; ++i) acc[ct][i] *= alpha;
      const bf16x8 p0 = pack_step(s, 0), p1 = pack_step(s, 1);
#pragma unroll
      for (int ct = 0; ct < 4; ++ct) {
        bf16x8 a0, a1;
        tr_read4(tb + trof[ct][0][0], tb + trof[ct][0][1], tb + trof[ct][1][0], tb + trof[ct][1][1], a0, a1);
        acc[ct] = MFMA32(a0, p0, acc[ct]);
        acc[ct] = MFMA32(a1, p1, acc[ct]);
      }
      if (t + RING_B < 8) issue(t + RING_B);
    }
    l_run += __shfl_xor(l_run, 32);
    float* ap = accB + ((size_t)wg * 32 + r) * 512 + w * 128;
#pragma unroll
    for (int ct = 0; ct < 4; ++ct)
#pragma unroll
      for (int g = 0; g < 4; ++g) {
        f32x4 v = {acc[ct][4 * g], acc[ct][4 * g + 1], acc[ct][4 * g + 2], acc[ct][4 * g + 3]};
        *(f32x4*)(ap + ct * 32 + 8 * g + 4 * h) = v;
      }
    if (w == 0 && lane < 32) { mlB[(wg * 32 + r) * 2] = m_run; mlB[(wg * 32 + r) * 2 + 1] = l_run; }
  } else {
    const int wg = blockIdx.x - 128, bk = wg >> 2, quarter = wg & 3;
    const int b = bk >> 3, kvh = bk & 7;
    const int split = quarter * 4 + w;
    const int key0 = SK - WIN + split * KEYS_A;
    const u16* ksrc = kc + ((size_t)bk * SK + key0) * DH;
    const u16* vsrc = vc + ((size_t)bk * SK + key0) * DH;
    const unsigned ring = sbase + w * 32768;
    unsigned goff[8];
#pragma unroll
    for (int i = 0; i < 8; ++i) {
      const unsigned n = lane + 64 * i, row = n >> 4, ch = (n & 15) ^ xr_b(row);
      goff[i] = row * DH + ch * 8;
    }
    auto issue = [&](int t) {
      const unsigned dst = ring + (t & 1) * 16384;
#pragma unroll
      for (int i = 0; i < 8; ++i) glds16(ksrc + (size_t)t * 32 * DH + goff[i], dst + i * 1024);
#pragma unroll
      for (int i = 0; i < 8; ++i) glds16(vsrc + (size_t)t * 32 * DH + goff[i], dst + 8192 + i * 1024);
    };
    issue(0); issue(1);
    bf16x8 qf[8];
#pragma unroll
    for (int s = 0; s < 8; ++s) {
      bf16x8 z = {0, 0, 0, 0, 0, 0, 0, 0};
      qf[s] = z;
      if (r < 4) qf[s] = *(const bf16x8*)(qA + b * 4096 + (kvh * 4 + r) * 128 + 16 * s + 8 * h);
    }
#pragma unroll
    for (int t = 0; t < 4; ++t) {
      if (t == 0) { WAIT_VM(0); } else if (t < 3) { WAIT_VM(16); } else { WAIT_VM(0); }
      const unsigned tb = ring + (t & 1) * 16384;
      f32x16 s;
#pragma unroll
      for (int i = 0; i < 16; ++i) s[i] = 0.f;
#pragma unroll
      for (int ks = 0; ks < 8; ++ks) {
        const bf16x8 a = *(const bf16x8*)(smem + (tb - sbase) + rowof[ks]);
        s = MFMA32(a, qf[ks], s);
      }
#pragma unroll
      for (int i = 0; i < 16; ++i) s[i] *= scale;
      float mx = s[0];
#pragma unroll
      for (int i = 1; i < 16; ++i) mx = fmaxf(mx, s[i]);
      mx = fmaxf(mx, __shfl_xor(mx, 32));
      const float m_new = fmaxf(m_run, mx);
      const float alpha = exp2f(m_run - m_new);
      m_run = m_new;
      float ps = 0.f;
#pragma unroll
      for (int i = 0; i < 16; ++i) { s[i] = exp2f(s[i] - m_new); ps += s[i]; }
      l_run = l_run * alpha + ps;
#pragma unroll
      for (int ct = 0; ct < 4; ++ct)
#pragma unroll
        for (int i = 0; i < 16; ++i) acc[ct][i] *= alpha;
      const bf16x8 p0 = pack_step(s, 0), p1 = pack_step(s, 1);
#pragma unroll
      for (int ct = 0; ct < 4; ++ct) {
        bf16x8 a0, a1;
        tr_read4(tb + 8192 + trof[ct][0][0], tb + 8192 + trof[ct][0][1], tb + 8192 + trof[ct][1][0], tb + 8192 + trof[ct][1][1], a0, a1);
        acc[ct] = MFMA32(a0, p0, acc[ct]);
        acc[ct] = MFMA32(a1, p1, acc[ct]);
      }
      if (t + 2 < 4) issue(t + 2);
    }
    l_run += __shfl_xor(l_run, 32);
    const int pw = bk * NSPLIT_A + split;
    if (r < 4) {
      float* ap = accA + ((size_t)pw * 4 + r) * 128;
#pragma unroll
      for (int ct = 0; ct < 4; ++ct)
#pragma unroll
        for (int g = 0; g < 4; ++g) {
          f32x4 v = {acc[ct][4 * g], acc[ct][4 * g + 1], acc[ct][4 * g + 2], acc[ct][4 * g + 3]};
          *(f32x4*)(ap + ct * 32 + 8 * g + 4 * h) = v;
        }
      if (h == 0) { mlA[(pw * 4 + r) * 2] = m_run; mlA[(pw * 4 + r) * 2 + 1] = l_run; }
    }
  }
}

__global__ __launch_bounds__(256) void k3_proj(const float* __restrict__ accB, const float* __restrict__ mlB,
                                                const float* __restrict__ accA, const float* __restrict__ mlA,
                                                const u16* __restrict__ wuv, const u16* __restrict__ woB,
                                                const u16* __restrict__ woA, float* __restrict__ opart) {
  __shared__ float wgt[4][32];
  __shared__ float vec[4][128];
  __shared__ float red[4][4][512];
  const int tid = threadIdx.x, lane = tid & 63, w = tid >> 6;
  int nrows; const u16* wo;
  if (blockIdx.x < 256) {
    const int h = blockIdx.x >> 3, cs = blockIdx.x & 7;
    if (tid < 128) {
      const int b = tid >> 5, sp = tid & 31;
      const float m = mlB[(((b * 32 + sp) * 32) + h) * 2], l = mlB[(((b * 32 + sp) * 32) + h) * 2 + 1];
      float M = m;
#pragma unroll
      for (int o = 1; o < 32; o <<= 1) M = fmaxf(M, __shfl_xor(M, o));
      const float e = exp2f(m - M);
      float L = e * l;
#pragma unroll
      for (int o = 1; o < 32; o <<= 1) L += __shfl_xor(L, o);
      wgt[b][sp] = e / L;
    }
    __syncthreads();
    float cl;
    {
      const int b = tid >> 6, c = cs * 64 + (tid & 63);
      float s = 0.f;
#pragma unroll 8
      for (int sp = 0; sp < 32; ++sp) s += wgt[b][sp] * accB[(((size_t)(b * 32 + sp) * 32) + h) * 512 + c];
      cl = s;
    }
    __shared__ float cls[4][64];
    cls[tid >> 6][tid & 63] = cl;
    __syncthreads();
    {
      const int rg = tid >> 4, cg = tid & 15;
      float acc[4][8];
#pragma unroll
      for (int b = 0; b < 4; ++b)
#pragma unroll
        for (int j = 0; j < 8; ++j) acc[b][j] = 0.f;
#pragma unroll
      for (int i = 0; i < 4; ++i) {
        const int row = i * 16 + rg;
        const u32x4 v = *(const u32x4*)(wuv + (size_t)(cs * 64 + row) * 4096 + h * 128 + cg * 8);
#pragma unroll
        for (int b = 0; b < 4; ++b) {
          const float xv = cls[b][row];
#pragma unroll
          for (int j = 0; j < 4; ++j) { acc[b][2 * j] += xv * bfl(v[j]); acc[b][2 * j + 1] += xv * bfh(v[j]); }
        }
      }
#pragma unroll
      for (int b = 0; b < 4; ++b)
#pragma unroll
        for (int j = 0; j < 8; ++j) {
          float s = acc[b][j];
          s += __shfl_xor(s, 16); s += __shfl_xor(s, 32);
          if (lane < 16) red[w][b][cg * 8 + j] = s;
        }
    }
    __syncthreads();
    for (int i = tid; i < 512; i += 256) {
      const int b = i >> 7, d = i & 127;
      vec[b][d] = red[0][b][d] + red[1][b][d] + red[2][b][d] + red[3][b][d];
    }
    __syncthreads();
    nrows = 128; wo = woB + (size_t)h * 128 * 512;
  } else {
    const int js = blockIdx.x - 256;
    const int hq = js >> 1, d0 = (js & 1) * 64;
    const int kvh = hq >> 2, hr = hq & 3;
    if (tid < 64) {
      const int b = tid >> 4, sp = tid & 15;
      const int pw = (b * 8 + kvh) * 16 + sp;
      const float m = mlA[(pw * 4 + hr) * 2], l = mlA[(pw * 4 + hr) * 2 + 1];
      float M = m;
#pragma unroll
      for (int o = 1; o < 16; o <<= 1) M = fmaxf(M, __shfl_xor(M, o));
      const float e = exp2f(m - M);
      float L = e * l;
#pragma unroll
      for (int o = 1; o < 16; o <<= 1) L += __shfl_xor(L, o);
      wgt[b][sp] = e / L;
    }
    __syncthreads();
    {
      const int b = tid >> 6, d = tid & 63;
      float s = 0.f;
#pragma unroll
      for (int sp = 0; sp < 16; ++sp) s += wgt[b][sp] * accA[((size_t)(((b * 8 + kvh) * 16 + sp) * 4 + hr)) * 128 + d0 + d];
      vec[b][d] = s;
    }
    __syncthreads();
    nrows = 64; wo = woA + (size_t)js * 64 * 512;
  }
  const int rw = nrows >> 2;
  float acc[4][8];
#pragma unroll
  for (int b = 0; b < 4; ++b)
#pragma unroll
    for (int j = 0; j < 8; ++j) acc[b][j] = 0.f;
#pragma unroll 16
  for (int i = 0; i < rw; ++i) {
    const int row = w * rw + i;
    const u32x4 v = *(const u32x4*)(wo + (size_t)row * 512 + lane * 8);
#pragma unroll
    for (int b = 0; b < 4; ++b) {
      const float xv = vec[b][row];
#pragma unroll
      for (int j = 0; j < 4; ++j) { acc[b][2 * j] += xv * bfl(v[j]); acc[b][2 * j + 1] += xv * bfh(v[j]); }
    }
  }
  __syncthreads();
#pragma unroll
  for (int b = 0; b < 4; ++b)
#pragma unroll
    for (int j = 0; j < 8; ++j) red[w][b][lane * 8 + j] = acc[b][j];
  __syncthreads();
  for (int i = tid; i < 2048; i += 256) {
    const int b = i >> 9, c = i & 511;
    opart[(size_t)blockIdx.x * 2048 + i] = red[0][b][c] + red[1][b][c] + red[2][b][c] + red[3][b][c];
  }
}

__global__ __launch_bounds__(256) void k4_final(const float* __restrict__ opart, u16* __restrict__ out) {
  const int o = blockIdx.x * 64 + (threadIdx.x & 63), part = threadIdx.x >> 6;
  const int b = o >> 10, c = o & 1023;
  __shared__ float red[4][64];
  float s = 0.f;
  if (c < 512) {
    for (int i = part; i < 64; i += 4) s += opart[(size_t)(256 + i) * 2048 + b * 512 + c];
  } else {
    for (int i = part; i < 256; i += 4) s += opart[(size_t)i * 2048 + b * 512 + (c - 512)];
  }
  red[part][threadIdx.x & 63] = s;
  __syncthreads();
  if (threadIdx.x < 64) out[o] = f2bf(red[0][threadIdx.x] + red[1][threadIdx.x] + red[2][threadIdx.x] + red[3][threadIdx.x]);
}

extern "C" void kernel_launch(void* const* d_in, const int* in_sizes, int n_in,
                              void* d_out, int out_size, void* d_ws, size_t ws_size,
                              hipStream_t stream) {
  const u16* xA  = (const u16*)d_in[0];
  const u16* kc  = (const u16*)d_in[1];
  const u16* vc  = (const u16*)d_in[2];
  const u16* wq  = (const u16*)d_in[3];
  const u16* woA = (const u16*)d_in[4];
  const u16* xB  = (const u16*)d_in[5];
  const u16* ckv = (const u16*)d_in[6];
  const u16* wqB = (const u16*)d_in[7];
  const u16* wuk = (const u16*)d_in[8];
  const u16* wuv = (const u16*)d_in[9];
  const u16* woB = (const u16*)d_in[10];
  char* ws = (char*)d_ws;
  u16* qA = (u16*)(ws + OFF_QA);
  u16* qabs = (u16*)(ws + OFF_QABS);
  float* accB = (float*)(ws + OFF_ACCB);
  float* mlB = (float*)(ws + OFF_MLB);
  float* accA = (float*)(ws + OFF_ACCA);
  float* mlA = (float*)(ws + OFF_MLA);
  float* opart = (float*)(ws + OFF_OP);
  const int lds2 = 4 * RING_B * 8192 + 2 * 16384;
  hipFuncSetAttribute((const void*)k2_attn, hipFuncAttributeMaxDynamicSharedMemorySize, lds2);
  k1_qproj<<<320, 256, 0, stream>>>(xA, wq, xB, wqB, wuk, qA, qabs);
  k2_attn<<<256, 256, lds2, stream>>>(kc, vc, ckv, qA, qabs, accB, mlB, accA, mlA);
  k3_proj<<<320, 256, 0, stream>>>(accB, mlB, accA, mlA, wuv, woB, woA, opart);
  k4_final<<<64, 256, 0, stream>>>(opart, (u16*)d_out);
}
```

```cpp
#include <hip/hip_runtime.h>
#include <hip/hip_bf16.h>
#include <stdint.h>

typedef unsigned short u16;
using bf16x8 = __attribute__((ext_vector_type(8))) short;
using s16x4  = __attribute__((ext_vector_type(4))) short;
using f32x16 = __attribute__((ext_vector_type(16))) float;
using f32x4  = __attribute__((ext_vector_type(4))) float;
using u32x4  = __attribute__((ext_vector_type(4))) unsigned;
#define DI __device__ __forceinline__
#define MFMA32(a, b, c) __builtin_amdgcn_mfma_f32_32x32x16_bf16((a), (b), (c), 0, 0, 0)

constexpr int NB = 4;
constexpr int DM = 512;
constexpr int HQ = 32, HKV = 8, SK = 8192, DH = 128, WIN = 2048;
constexpr int DC = 512;
constexpr int NSPLIT_B = 32;
constexpr int KEYS_B = SK / NSPLIT_B;
constexpr int NSPLIT_A = 32;
constexpr int KEYS_A = WIN / NSPLIT_A;

constexpr size_t OFF_QA   = 0;
constexpr size_t OFF_QABS = OFF_QA + 4 * 4096 * 2;
constexpr size_t OFF_ACCB = OFF_QABS + 4 * 32 * 512 * 2;
constexpr size_t OFF_MLB  = OFF_ACCB + (size_t)128 * 32 * 512 * 4;
constexpr size_t OFF_ACCA = OFF_MLB + 128 * 32 * 2 * 4;
constexpr size_t OFF_MLA  = OFF_ACCA + (size_t)1024 * 4 * 128 * 4;
constexpr size_t OFF_OP   = OFF_MLA + 1024 * 4 * 2 * 4;

DI float bf2f(u16 x) { return __uint_as_float(((unsigned)x) << 16); }
DI float bfl(unsigned x) { return __uint_as_float(x << 16); }
DI float bfh(unsigned x) { return __uint_as_float(x & 0xffff0000u); }
DI u16 f2bf(float x) { unsigned u = __float_as_uint(x); u += 0x7fffu + ((u >> 16) & 1u); return (u16)(u >> 16); }
DI float rbf(float x) { return bf2f(f2bf(x)); }

DI unsigned xr_b(unsigned row) { return ((row & 3) << 2) | ((row >> 2) & 3); }
DI unsigned off_b(unsigned row, unsigned ch) { return 256u * row + 16u * (ch ^ xr_b(row)); }

DI void glds16(const void* gsrc, unsigned lds_dst) {
  unsigned keep;
  asm volatile("s_mov_b32 %0, m0\n\ts_mov_b32 m0, %2\n\ts_nop 0\n\tglobal_load_lds_dwordx4 %1, off nt\n\ts_mov_b32 m0, %0"
               : "=&s"(keep) : "v"(gsrc), "s"(lds_dst) : "memory");
}
#define WAIT_VM(n) asm volatile("s_waitcnt vmcnt(" #n ")" ::: "memory")

DI bf16x8 pack_step(const f32x16& x, int s) {
  u32x4 p;
  asm volatile("s_nop 1\n\tv_cvt_pk_bf16_f32 %0, %4, %5\n\tv_cvt_pk_bf16_f32 %1, %6, %7\n\tv_cvt_pk_bf16_f32 %2, %8, %9\n\tv_cvt_pk_bf16_f32 %3, %10, %11\n\ts_nop 1"
               : "=&v"(p[0]), "=&v"(p[1]), "=&v"(p[2]), "=&v"(p[3])
               : "v"(x[8 * s]), "v"(x[8 * s + 1]), "v"(x[8 * s + 2]), "v"(x[8 * s + 3]), "v"(x[8 * s + 4]), "v"(x[8 * s + 5]), "v"(x[8 * s + 6]), "v"(x[8 * s + 7]));
  return __builtin_bit_cast(bf16x8, p);
}

DI void tr_read4(unsigned a0, unsigned a1, unsigned a2, unsigned a3, bf16x8& f0, bf16x8& f1) {
  s16x4 r0, r1, r2, r3;
  asm volatile("ds_read_b64_tr_b16 %0, %4\n\tds_read_b64_tr_b16 %1, %5\n\tds_read_b64_tr_b16 %2, %6\n\tds_read_b64_tr_b16 %3, %7\n\ts_waitcnt lgkmcnt(0)"
               : "=&v"(r0), "=&v"(r1), "=&v"(r2), "=&v"(r3) : "v"(a0), "v"(a1), "v"(a2), "v"(a3) : "memory");
  f0 = __builtin_shufflevector(r0, r1, 0, 1, 2, 3, 4, 5, 6, 7);
  f1 = __builtin_shufflevector(r2, r3, 0, 1, 2, 3, 4, 5, 6, 7);
}

__global__ __launch_bounds__(256) void k1_qproj(const u16* __restrict__ xA, const u16* __restrict__ wq,
                                                 const u16* __restrict__ xB, const u16* __restrict__ wqB,
                                                 const u16* __restrict__ wuk, u16* __restrict__ qA, u16* __restrict__ qabs) {
  __shared__ float xs[4][512];
  __shared__ __attribute__((aligned(16))) float red[8448];
  __shared__ float qb[4][128];
  const int tid = threadIdx.x, lane = tid & 63, w = tid >> 6;
  const bool partA = blockIdx.x < 64;
  const u16* x = partA ? xA : xB;
  const u32x4 xv4 = *(const u32x4*)(x + tid * 8);
  auto stage_x = [&]() {
    float* d = &xs[0][0] + tid * 8;
#pragma unroll
    for (int j = 0; j < 4; ++j) { d[2 * j] = bfl(xv4[j]); d[2 * j + 1] = bfh(xv4[j]); }
    __syncthreads();
  };
  if (partA) {
    const int col0 = blockIdx.x * 64;
    const int rg = lane >> 3, cg = lane & 7;
    float acc[4][8];
#pragma unroll
    for (int b = 0; b < 4; ++b)
#pragma unroll
      for (int j = 0; j < 8; ++j) acc[b][j] = 0.f;
    u32x4 wv[16];
#pragma unroll
    for (int i = 0; i < 16; ++i) wv[i] = __builtin_nontemporal_load((const u32x4*)(wq + (size_t)(w * 128 + i * 8 + rg) * 4096 + col0 + cg * 8));
    stage_x();
    __builtin_amdgcn_sched_barrier(0);
#pragma unroll
    for (int i = 0; i < 16; ++i) {
      const int row = w * 128 + i * 8 + rg;
      const u32x4 v = wv[i];
#pragma unroll
      for (int b = 0; b < 4; ++b) {
        const float xv = xs[b][row];
#pragma unroll
        for (int j = 0; j < 4; ++j) { acc[b][2 * j] += xv * bfl(v[j]); acc[b][2 * j + 1] += xv * bfh(v[j]); }
      }
    }
    const int grp = w * 8 + rg;
#pragma unroll
    for (int b = 0; b < 4; ++b) {
      *(f32x4*)&red[grp * 260 + b * 64 + cg * 8] = f32x4{acc[b][0], acc[b][1], acc[b][2], acc[b][3]};
      *(f32x4*)&red[grp * 260 + b * 64 + cg * 8 + 4] = f32x4{acc[b][4], acc[b][5], acc[b][6], acc[b][7]};
    }
    __syncthreads();
    {
      float s = 0.f;
#pragma unroll
      for (int g = 0; g < 32; ++g) s += red[g * 260 + tid];
      qA[(tid >> 6) * 4096 + col0 + (tid & 63)] = f2bf(s);
    }
  } else {
    const int id = blockIdx.x - 64, h = id & 31, cs = id >> 5;
    const int rg = tid >> 4, cg = tid & 15;
    u32x4 uk[4];
#pragma unroll
    for (int i = 0; i < 4; ++i) uk[i] = __builtin_nontemporal_load((const u32x4*)(wuk + (size_t)(cs * 64 + i * 16 + rg) * 4096 + h * 128 + cg * 8));
    float acc[4][8];
#pragma unroll
    for (int b = 0; b < 4; ++b)
#pragma unroll
      for (int j = 0; j < 8; ++j) acc[b][j] = 0.f;
    u32x4 wv[32];
#pragma unroll
    for (int i = 0; i < 32; ++i) wv[i] = *(const u32x4*)(wqB + (size_t)(i * 16 + rg) * 4096 + h * 128 + cg * 8);
    stage_x();
    __builtin_amdgcn_sched_barrier(0);
#pragma unroll
    for (int i = 0; i < 32; ++i) {
      const int row = i * 16 + rg;
      const u32x4 v = wv[i];
#pragma unroll
      for (int b = 0; b < 4; ++b) {
        const float xv = xs[b][row];
#pragma unroll
        for (int j = 0; j < 4; ++j) { acc[b][2 * j] += xv * bfl(v[j]); acc[b][2 * j + 1] += xv * bfh(v[j]); }
      }
    }
#pragma unroll
    for (int b = 0; b < 4; ++b) {
      *(f32x4*)&red[rg * 516 + b * 128 + cg * 8] = f32x4{acc[b][0], acc[b][1], acc[b][2], acc[b][3]};
      *(f32x4*)&red[rg * 516 + b * 128 + cg * 8 + 4] = f32x4{acc[b][4], acc[b][5], acc[b][6], acc[b][7]};
    }
    __syncthreads();
#pragma unroll
    for (int k = 0; k < 2; ++k) {
      const int i = tid + k * 256;
      float s = 0.f;
#pragma unroll
      for (int g = 0; g < 16; ++g) s += red[g * 516 + i];
      (&qb[0][0])[i] = rbf(s);
    }
    __syncthreads();
#pragma unroll
    for (int i = 0; i < 4; ++i) {
      const u32x4 v = uk[i];
#pragma unroll
      for (int b = 0; b < 4; ++b) {
        float s = 0.f;
#pragma unroll
        for (int j = 0; j < 4; ++j) { s += qb[b][cg * 8 + 2 * j] * bfl(v[j]); s += qb[b][cg * 8 + 2 * j + 1] * bfh(v[j]); }
        red[(i * 16 + rg) * 68 + b * 17 + cg] = s;
      }
    }
    __syncthreads();
    {
      const int row = tid >> 2, b = tid & 3;
      float s = 0.f;
#pragma unroll
      for (int g = 0; g < 16; ++g) s += red[row * 68 + b * 17 + g];
      qabs[(b * 32 + h) * 512 + cs * 64 + row] = f2bf(s);
    }
  }
}

__global__ __launch_bounds__(512) void k2_attn(const u16* __restrict__ kc, const u16* __restrict__ vc,
                                                const u16* __restrict__ ckv, const u16* __restrict__ qA,
                                                const u16* __restrict__ qabs, float* __restrict__ accB,
                                                float* __restrict__ mlB, float* __restrict__ accA, float* __restrict__ mlA) {
  extern __shared__ __attribute__((aligned(16))) char smem[];
  const int tid = threadIdx.x, lane = tid & 63;
  const int w8 = __builtin_amdgcn_readfirstlane(tid >> 6);
  const int g = w8 >> 2, w = w8 & 3;
  const int r = lane & 31, h = lane >> 5;
  const unsigned sbase = (unsigned)(size_t)smem;
  const float LOG2E = 1.4426950408889634f;
  const float scale = 0.08838834764831845f * LOG2E;

  const unsigned blk = (lane >> 4) & 1, q4 = (lane & 15) >> 2, p = lane & 3;
  unsigned trof[4][2][2];
#pragma unroll
  for (int ct = 0; ct < 4; ++ct)
#pragma unroll
    for (int ks = 0; ks < 2; ++ks)
#pragma unroll
      for (int t = 0; t < 2; ++t)
        trof[ct][ks][t] = off_b(16 * ks + 8 * t + 4 * h + q4, 4 * ct + 2 * blk + (p >> 1)) + 8 * (p & 1);
  unsigned rowof[8];
#pragma unroll
  for (int s = 0; s < 8; ++s) rowof[s] = off_b(r, 2 * s + h);

  f32x16 acc[4];
#pragma unroll
  for (int ct = 0; ct < 4; ++ct)
#pragma unroll
    for (int i = 0; i < 16; ++i) acc[ct][i] = 0.f;
  float m_run = -1e30f, l_run = 0.f;
  const unsigned ring = sbase + w8 * 16384;

  if (blockIdx.x < 128) {
    const int wg = blockIdx.x, b = wg >> 5, sp = wg & 31;
    const int key0 = sp * KEYS_B + g * 32;
    const u16* src = ckv + ((size_t)b * SK + key0) * DC + w * 128;
    const unsigned sx = sbase + 8 * 16384 + g * 16384;
    unsigned goff[8];
#pragma unroll
    for (int i = 0; i < 8; ++i) {
      const unsigned n = lane + 64 * i, row = n >> 4, ch = (n & 15) ^ xr_b(row);
      goff[i] = row * DC + ch * 8;
    }
    auto issue = [&](int t) {
      const u16* tp = src + (size_t)t * 64 * DC;
      const unsigned dst = ring + (t & 1) * 8192;
#pragma unroll
      for (int i = 0; i < 8; ++i) glds16(tp + goff[i], dst + i * 1024);
    };
    issue(0); issue(1);
    bf16x8 qf[8];
#pragma unroll
    for (int s = 0; s < 8; ++s) qf[s] = *(const bf16x8*)(qabs + (b * 32 + r) * 512 + w * 128 + 16 * s + 8 * h);

#pragma unroll
    for (int t = 0; t < 4; ++t) {
      if (t == 0 || t == 3) { WAIT_VM(0); } else { WAIT_VM(8); }
      const unsigned tb = ring + (t & 1) * 8192;
      f32x16 s;
#pragma unroll
      for (int i = 0; i < 16; ++i) s[i] = 0.f;
#pragma unroll
      for (int ks = 0; ks < 8; ++ks) {
        const bf16x8 a = *(const bf16x8*)(smem + (tb - sbase) + rowof[ks]);
        s = MFMA32(a, qf[ks], s);
      }
      if (t > 0) { __builtin_amdgcn_s_barrier(); asm volatile("" ::: "memory"); }
#pragma unroll
      for (int gg = 0; gg < 4; ++gg) {
        f32x4 v = {s[4 * gg], s[4 * gg + 1], s[4 * gg + 2], s[4 * gg + 3]};
        *(f32x4*)(smem + (sx - sbase) + w * 4096 + gg * 1024 + lane * 16) = v;
      }
      asm volatile("s_waitcnt lgkmcnt(0)" ::: "memory");
      __builtin_amdgcn_s_barrier();
      asm volatile("" ::: "memory");
#pragma unroll
      for (int gg = 0; gg < 4; ++gg) {
        f32x4 v0 = *(const f32x4*)(smem + (sx - sbase) + 0 * 4096 + gg * 1024 + lane * 16);
        f32x4 v1 = *(const f32x4*)(smem + (sx - sbase) + 1 * 4096 + gg * 1024 + lane * 16);
        f32x4 v2 = *(const f32x4*)(smem + (sx - sbase) + 2 * 4096 + gg * 1024 + lane * 16);
        f32x4 v3 = *(const f32x4*)(smem + (sx - sbase) + 3 * 4096 + gg * 1024 + lane * 16);
#pragma unroll
        for (int j = 0; j < 4; ++j) s[4 * gg + j] = ((v0[j] + v1[j]) + (v2[j] + v3[j])) * scale;
      }
      float mx = s[0];
#pragma unroll
      for (int i = 1; i < 16; ++i) mx = fmaxf(mx, s[i]);
      mx = fmaxf(mx, __shfl_xor(mx, 32));
      const float m_new = fmaxf(m_run, mx);
      const float alpha = exp2f(m_run - m_new);
      m_run = m_new;
      float ps = 0.f;
#pragma unroll
      for (int i = 0; i < 16; ++i) { s[i] = exp2f(s[i] - m_new); ps += s[i]; }
      l_run = l_run * alpha + ps;
#pragma unroll
      for (int ct = 0; ct < 4; ++ct)
#pragma unroll
        for (int i = 0; i < 16; ++i) acc[ct][i] *= alpha;
      const bf16x8 p0 = pack_step(s, 0), p1 = pack_step(s, 1);
#pragma unroll
      for (int ct = 0; ct < 4; ++ct) {
        bf16x8 a0, a1;
        tr_read4(tb + trof[ct][0][0], tb + trof[ct][0][1], tb + trof[ct][1][0], tb + trof[ct][1][1], a0, a1);
        acc[ct] = MFMA32(a0, p0, acc[ct]);
        acc[ct] = MFMA32(a1, p1, acc[ct]);
      }
      if (t + 2 < 4) issue(t + 2);
    }
    l_run += __shfl_xor(l_run, 32);
    float* mlx = (float*)(smem + 8 * 16384);
    __builtin_amdgcn_s_barrier();
    asm volatile("" ::: "memory");
    if (g == 1) {
#pragma unroll
      for (int ct = 0; ct < 4; ++ct)
#pragma unroll
        for (int gg = 0; gg < 4; ++gg) {
          f32x4 v = {acc[ct][4 * gg], acc[ct][4 * gg + 1], acc[ct][4 * gg + 2], acc[ct][4 * gg + 3]};
          *(f32x4*)(smem + (ring - sbase) + (ct * 4 + gg) * 1024 + lane * 16) = v;
        }
      if (w == 0) { mlx[lane] = m_run; mlx[64 + lane] = l_run; }
    }
    asm volatile("s_waitcnt lgkmcnt(0)" ::: "memory");
    __builtin_amdgcn_s_barrier();
    asm volatile("" ::: "memory");
    if (g == 0) {
      const float m1 = mlx[lane], l1 = mlx[64 + lane];
      const float mm = fmaxf(m_run, m1);
      const float a0 = exp2f(m_run - mm), a1 = exp2f(m1 - mm);
      l_run = l_run * a0 + l1 * a1; m_run = mm;
      const unsigned oring = ring + 4 * 16384;
#pragma unroll
      for (int ct = 0; ct < 4; ++ct)
#pragma unroll
        for (int gg = 0; gg < 4; ++gg) {
          const f32x4 v = *(const f32x4*)(smem + (oring - sbase) + (ct * 4 + gg) * 1024 + lane * 16);
#pragma unroll
          for (int j = 0; j < 4; ++j) acc[ct][4 * gg + j] = acc[ct][4 * gg + j] * a0 + v[j] * a1;
        }
#pragma unroll
      for (int ct = 0; ct < 4; ++ct)
#pragma unroll
        for (int gg = 0; gg < 4; ++gg) {
          f32x4 v = {acc[ct][4 * gg], acc[ct][4 * gg + 1], acc[ct][4 * gg + 2], acc[ct][4 * gg + 3]};
          *(f32x4*)(smem + (ring - sbase) + r * 512 + (((ct * 32 + 8 * gg + 4 * h) * 4) ^ ((r & 7) << 4))) = v;
        }
      asm volatile("s_waitcnt lgkmcnt(0)" ::: "memory");
      float* ap = accB + ((size_t)wg * 32) * 512 + w * 128;
#pragma unroll
      for (int i = 0; i < 16; ++i) {
        const int hd = i * 2 + h, c4 = r;
        const f32x4 v = *(const f32x4*)(smem + (ring - sbase) + hd * 512 + ((c4 * 16) ^ ((hd & 7) << 4)));
        *(f32x4*)(ap + (size_t)hd * 512 + c4 * 4) = v;
      }
      if (w == 0 && lane < 32) { mlB[(wg * 32 + r) * 2] = m_run; mlB[(wg * 32 + r) * 2 + 1] = l_run; }
    }
  } else {
    const int wg = blockIdx.x - 128, bk = wg >> 2, quarter = wg & 3;
    const int b = bk >> 3, kvh = bk & 7;
    const int split = quarter * 8 + w8;
    const int key0 = SK - WIN + split * KEYS_A;
    const u16* ksrc = kc + ((size_t)bk * SK + key0) * DH;
    const u16* vsrc = vc + ((size_t)bk * SK + key0) * DH;
    unsigned goff[8];
#pragma unroll
    for (int i = 0; i < 8; ++i) {
      const unsigned n = lane + 64 * i, row = n >> 4, ch = (n & 15) ^ xr_b(row);
      goff[i] = row * DH + ch * 8;
    }
    auto issueK = [&](int t) {
#pragma unroll
      for (int i = 0; i < 8; ++i) glds16(ksrc + (size_t)t * 32 * DH + goff[i], ring + i * 1024);
    };
    auto issueV = [&](int t) {
#pragma unroll
      for (int i = 0; i < 8; ++i) glds16(vsrc + (size_t)t * 32 * DH + goff[i], ring + 8192 + i * 1024);
    };
    bf16x8 qf[8];
#pragma unroll
    for (int s = 0; s < 8; ++s) {
      bf16x8 z = {0, 0, 0, 0, 0, 0, 0, 0};
      qf[s] = z;
      if (r < 4) qf[s] = *(const bf16x8*)(qA + b * 4096 + (kvh * 4 + r) * 128 + 16 * s + 8 * h);
    }
    issueK(0); issueV(0);
#pragma unroll
    for (int t = 0; t < 2; ++t) {
      WAIT_VM(8);
      f32x16 s;
#pragma unroll
      for (int i = 0; i < 16; ++i) s[i] = 0.f;
#pragma unroll
      for (int ks = 0; ks < 8; ++ks) {
        const bf16x8 a = *(const bf16x8*)(smem + (ring - sbase) + rowof[ks]);
        s = MFMA32(a, qf[ks], s);
      }
      asm volatile("s_waitcnt lgkmcnt(0)" ::: "memory");
      if (t == 0) issueK(1);
#pragma unroll
      for (int i = 0; i < 16; ++i) s[i] *= scale;
      float mx = s[0];
#pragma unroll
      for (int i = 1; i < 16; ++i) mx = fmaxf(mx, s[i]);
      mx = fmaxf(mx, __shfl_xor(mx, 32));
      const float m_new = fmaxf(m_run, mx);
      const float alpha = exp2f(m_run - m_new);
      m_run = m_new;
      float ps = 0.f;
#pragma unroll
      for (int i = 0; i < 16; ++i) { s[i] = exp2f(s[i] - m_new); ps += s[i]; }
      l_run = l_run * alpha + ps;
#pragma unroll
      for (int ct = 0; ct < 4; ++ct)
#pragma unroll
        for (int i = 0; i < 16; ++i) acc[ct][i] *= alpha;
      const bf16x8 p0 = pack_step(s, 0), p1 = pack_step(s, 1);
      if (t == 0) { WAIT_VM(8); } else { WAIT_VM(0); }
#pragma unroll
      for (int ct = 0; ct < 4; ++ct) {
        bf16x8 a0, a1;
        tr_read4(ring + 8192 + trof[ct][0][0], ring + 8192 + trof[ct][0][1], ring + 8192 + trof[ct][1][0], ring + 8192 + trof[ct][1][1], a0, a1);
        acc[ct] = MFMA32(a0, p0, acc[ct]);
        acc[ct] = MFMA32(a1, p1, acc[ct]);
      }
      if (t == 0) issueV(1);
    }
    l_run += __shfl_xor(l_run, 32);
    const int pw = bk * NSPLIT_A + split;
    if (r < 4) {
      float* ap = accA + ((size_t)pw * 4 + r) * 128;
#pragma unroll
      for (int ct = 0; ct < 4; ++ct)
#pragma unroll
        for (int gg = 0; gg < 4; ++gg) {
          f32x4 v = {acc[ct][4 * gg], acc[ct][4 * gg + 1], acc[ct][4 * gg + 2], acc[ct][4 * gg + 3]};
          *(f32x4*)(ap + ct * 32 + 8 * gg + 4 * h) = v;
        }
      if (h == 0) { mlA[(pw * 4 + r) * 2] = m_run; mlA[(pw * 4 + r) * 2 + 1] = l_run; }
    }
  }
}

__global__ __launch_bounds__(256) void k3_proj(const float* __restrict__ accB, const float* __restrict__ mlB,
                                                const float* __restrict__ accA, const float* __restrict__ mlA,
                                                const u16* __restrict__ wuv, const u16* __restrict__ woB,
                                                const u16* __restrict__ woA, float* __restrict__ opart) {
  __shared__ float wgt[4][32];
  __shared__ float vec[4][128];
  __shared__ float cls[4][64];
  __shared__ __attribute__((aligned(16))) float red[4][4][512];
  const int tid = threadIdx.x, lane = tid & 63, w = tid >> 6;
  float acc[4][8];
#pragma unroll
  for (int b = 0; b < 4; ++b)
#pragma unroll
    for (int j = 0; j < 8; ++j) acc[b][j] = 0.f;
  if (blockIdx.x < 256) {
    const int h = blockIdx.x & 31, cs = blockIdx.x >> 5;
    float pm = 0.f, pl = 0.f;
    if (tid < 128) {
      const int b = tid >> 5, sp = tid & 31;
      pm = mlB[(((b * 32 + sp) * 32) + h) * 2]; pl = mlB[(((b * 32 + sp) * 32) + h) * 2 + 1];
    }
    float pa[32];
    {
      const int b = tid >> 6, c = cs * 64 + (tid & 63);
#pragma unroll
      for (int sp = 0; sp < 32; ++sp) pa[sp] = __builtin_nontemporal_load(accB + (((size_t)(b * 32 + sp) * 32) + h) * 512 + c);
    }
    const int rg = tid >> 4, cg = tid & 15;
    u32x4 uv[4];
#pragma unroll
    for (int i = 0; i < 4; ++i) uv[i] = __builtin_nontemporal_load((const u32x4*)(wuv + (size_t)(cs * 64 + i * 16 + rg) * 4096 + h * 128 + cg * 8));
    u32x4 wv[32];
    const u16* wo = woB + (size_t)h * 128 * 512;
#pragma unroll
    for (int i = 0; i < 32; ++i) wv[i] = *(const u32x4*)(wo + (size_t)(w * 32 + i) * 512 + lane * 8);
    __builtin_amdgcn_sched_barrier(0);
    if (tid < 128) {
      const int b = tid >> 5, sp = tid & 31;
      float M = pm;
#pragma unroll
      for (int o = 1; o < 32; o <<= 1) M = fmaxf(M, __shfl_xor(M, o));
      const float e = exp2f(pm - M);
      float L = e * pl;
#pragma unroll
      for (int o = 1; o < 32; o <<= 1) L += __shfl_xor(L, o);
      wgt[b][sp] = e / L;
    }
    __syncthreads();
    {
      const int b = tid >> 6;
      float s = 0.f;
#pragma unroll
      for (int sp = 0; sp < 32; ++sp) s += wgt[b][sp] * pa[sp];
      cls[b][tid & 63] = s;
    }
    __syncthreads();
    {
      float a2[4][8];
#pragma unroll
      for (int b = 0; b < 4; ++b)
#pragma unroll
        for (int j = 0; j < 8; ++j) a2[b][j] = 0.f;
#pragma unroll
      for (int i = 0; i < 4; ++i) {
        const int row = i * 16 + rg;
        const u32x4 v = uv[i];
#pragma unroll
        for (int b = 0; b < 4; ++b) {
          const float xv = cls[b][row];
#pragma unroll
          for (int j = 0; j < 4; ++j) { a2[b][2 * j] += xv * bfl(v[j]); a2[b][2 * j + 1] += xv * bfh(v[j]); }
        }
      }
      float* rf = &red[0][0][0];
#pragma unroll
      for (int b = 0; b < 4; ++b) {
        *(f32x4*)&rf[rg * 512 + b * 128 + cg * 8] = f32x4{a2[b][0], a2[b][1], a2[b][2], a2[b][3]};
        *(f32x4*)&rf[rg * 512 + b * 128 + cg * 8 + 4] = f32x4{a2[b][4], a2[b][5], a2[b][6], a2[b][7]};
      }
    }
    __syncthreads();
#pragma unroll
    for (int k = 0; k < 2; ++k) {
      const int i = tid + k * 256;
      float s = 0.f;
#pragma unroll
      for (int g = 0; g < 16; ++g) s += (&red[0][0][0])[g * 512 + i];
      (&vec[0][0])[i] = s;
    }
    __syncthreads();
#pragma unroll
    for (int i = 0; i < 32; ++i) {
      const int row = w * 32 + i;
      const u32x4 v = wv[i];
#pragma unroll
      for (int b = 0; b < 4; ++b) {
        const float xv = vec[b][row];
#pragma unroll
        for (int j = 0; j < 4; ++j) { acc[b][2 * j] += xv * bfl(v[j]); acc[b][2 * j + 1] += xv * bfh(v[j]); }
      }
    }
  } else {
    const int js = blockIdx.x - 256;
    const int hq = js >> 1, d0 = (js & 1) * 64;
    const int kvh = hq >> 2, hr = hq & 3;
    float pm = 0.f, pl = 0.f;
    if (tid < 128) {
      const int b = tid >> 5, sp = tid & 31;
      const int pw = (b * 8 + kvh) * 32 + sp;
      pm = mlA[(pw * 4 + hr) * 2]; pl = mlA[(pw * 4 + hr) * 2 + 1];
    }
    float pa[32];
    {
      const int b = tid >> 6, d = tid & 63;
#pragma unroll
      for (int sp = 0; sp < 32; ++sp) pa[sp] = accA[((size_t)(((b * 8 + kvh) * 32 + sp) * 4 + hr)) * 128 + d0 + d];
    }
    u32x4 wv[16];
    const u16* wo = woA + (size_t)js * 64 * 512;
#pragma unroll
    for (int i = 0; i < 16; ++i) wv[i] = __builtin_nontemporal_load((const u32x4*)(wo + (size_t)(w * 16 + i) * 512 + lane * 8));
    __builtin_amdgcn_sched_barrier(0);
    if (tid < 128) {
      const int b = tid >> 5, sp = tid & 31;
      float M = pm;
#pragma unroll
      for (int o = 1; o < 32; o <<= 1) M = fmaxf(M, __shfl_xor(M, o));
      const float e = exp2f(pm - M);
      float L = e * pl;
#pragma unroll
      for (int o = 1; o < 32; o <<= 1) L += __shfl_xor(L, o);
      wgt[b][sp] = e / L;
    }
    __syncthreads();
    {
      const int b = tid >> 6, d = tid & 63;
      float s = 0.f;
#pragma unroll
      for (int sp = 0; sp < 32; ++sp) s += wgt[b][sp] * pa[sp];
      vec[b][d] = s;
    }
    __syncthreads();
#pragma unroll
    for (int i = 0; i < 16; ++i) {
      const int row = w * 16 + i;
      const u32x4 v = wv[i];
#pragma unroll
      for (int b = 0; b < 4; ++b) {
        const float xv = vec[b][row];
#pragma unroll
        for (int j = 0; j < 4; ++j) { acc[b][2 * j] += xv * bfl(v[j]); acc[b][2 * j + 1] += xv * bfh(v[j]); }
      }
    }
  }
  __syncthreads();
#pragma unroll
  for (int b = 0; b < 4; ++b)
#pragma unroll
    for (int j = 0; j < 8; ++j) red[w][b][lane * 8 + j] = acc[b][j];
  __syncthreads();
  for (int i = tid; i < 2048; i += 256) {
    const int b = i >> 9, c = i & 511;
    opart[(size_t)blockIdx.x * 2048 + i] = red[0][b][c] + red[1][b][c] + red[2][b][c] + red[3][b][c];
  }
}

__global__ __launch_bounds__(256) void k4_final(const float* __restrict__ opart, u16* __restrict__ out) {
  const int o = blockIdx.x * 16 + (threadIdx.x & 15), part = threadIdx.x >> 4;
  const int b = o >> 10, c = o & 1023;
  __shared__ float red[16][16];
  float s = 0.f;
  if (c < 512) {
    float v[4];
#pragma unroll
    for (int i = 0; i < 4; ++i) v[i] = opart[(size_t)(256 + part * 4 + i) * 2048 + b * 512 + c];
    s = (v[0] + v[1]) + (v[2] + v[3]);
  } else {
    float v[16];
#pragma unroll
    for (int i = 0; i < 16; ++i) v[i] = opart[(size_t)(part * 16 + i) * 2048 + b * 512 + (c - 512)];
#pragma unroll
    for (int i = 0; i < 16; ++i) s += v[i];
  }
  red[part][threadIdx.x & 15] = s;
  __syncthreads();
  if (threadIdx.x < 16) {
    float t = 0.f;
#pragma unroll
    for (int i = 0; i < 16; ++i) t += red[i][threadIdx.x];
    out[o] = f2bf(t);
  }
}

extern "C" void kernel_launch(void* const* d_in, const int* in_sizes, int n_in,
                              void* d_out, int out_size, void* d_ws, size_t ws_size,
                              hipStream_t stream) {
  const u16* xA  = (const u16*)d_in[0];
  const u16* kc  = (const u16*)d_in[1];
  const u16* vc  = (const u16*)d_in[2];
  const u16* wq  = (const u16*)d_in[3];
  const u16* woA = (const u16*)d_in[4];
  const u16* xB  = (const u16*)d_in[5];
  const u16* ckv = (const u16*)d_in[6];
  const u16* wqB = (const u16*)d_in[7];
  const u16* wuk = (const u16*)d_in[8];
  const u16* wuv = (const u16*)d_in[9];
  const u16* woB = (const u16*)d_in[10];
  char* ws = (char*)d_ws;
  u16* qA = (u16*)(ws + OFF_QA);
  u16* qabs = (u16*)(ws + OFF_QABS);
  float* accB = (float*)(ws + OFF_ACCB);
  float* mlB = (float*)(ws + OFF_MLB);
  float* accA = (float*)(ws + OFF_ACCA);
  float* mlA = (float*)(ws + OFF_MLA);
  float* opart = (float*)(ws + OFF_OP);
  const int lds2 = 163840;
  hipFuncSetAttribute((const void*)k2_attn, hipFuncAttributeMaxDynamicSharedMemorySize, lds2);
  k1_qproj<<<320, 256, 0, stream>>>(xA, wq, xB, wqB, wuk, qA, qabs);
  k2_attn<<<256, 512, lds2, stream>>>(kc, vc, ckv, qA, qabs, accB, mlB, accA, mlA);
  k3_proj<<<320, 256, 0, stream>>>(accB, mlB, accA, mlA, wuv, woB, woA, opart);
  k4_final<<<256, 256, 0, stream>>>(opart, (u16*)d_out);
}
```

```cpp
#include <hip/hip_runtime.h>
#include <hip/hip_bf16.h>
#include <stdint.h>

typedef unsigned short u16;
using bf16x8 = __attribute__((ext_vector_type(8))) short;
using s16x4  = __attribute__((ext_vector_type(4))) short;
using f32x16 = __attribute__((ext_vector_type(16))) float;
using f32x4  = __attribute__((ext_vector_type(4))) float;
using u32x4  = __attribute__((ext_vector_type(4))) unsigned;
#define DI __device__ __forceinline__
#define MFMA32(a, b, c) __builtin_amdgcn_mfma_f32_32x32x16_bf16((a), (b), (c), 0, 0, 0)

constexpr int NB = 4;
constexpr int DM = 512;
constexpr int HQ = 32, HKV = 8, SK = 8192, DH = 128, WIN = 2048;
constexpr int DC = 512;
constexpr int NSPLIT_B = 64;
constexpr int KEYS_B = SK / NSPLIT_B;
constexpr int NSPLIT_A = 32;
constexpr int KEYS_A = WIN / NSPLIT_A;

constexpr size_t OFF_QA   = 0;
constexpr size_t OFF_QABS = OFF_QA + 4 * 4096 * 2;
constexpr size_t OFF_ACCB = OFF_QABS + 4 * 32 * 512 * 2;
constexpr size_t OFF_MLB  = OFF_ACCB + (size_t)256 * 32 * 512 * 4;
constexpr size_t OFF_ACCA = OFF_MLB + 256 * 32 * 2 * 4;
constexpr size_t OFF_MLA  = OFF_ACCA + (size_t)1024 * 4 * 128 * 4;
constexpr size_t OFF_OP   = OFF_MLA + 1024 * 4 * 2 * 4;

DI float bf2f(u16 x) { return __uint_as_float(((unsigned)x) << 16); }
DI float bfl(unsigned x) { return __uint_as_float(x << 16); }
DI float bfh(unsigned x) { return __uint_as_float(x & 0xffff0000u); }
DI u16 f2bf(float x) { unsigned u = __float_as_uint(x); u += 0x7fffu + ((u >> 16) & 1u); return (u16)(u >> 16); }
DI float rbf(float x) { return bf2f(f2bf(x)); }

DI unsigned xr_b(unsigned row) { return ((row & 3) << 2) | ((row >> 2) & 3); }
DI unsigned off_b(unsigned row, unsigned ch) { return 256u * row + 16u * (ch ^ xr_b(row)); }

DI void glds16(const void* gsrc, unsigned lds_dst) {
  unsigned keep;
  asm volatile("s_mov_b32 %0, m0\n\ts_mov_b32 m0, %2\n\ts_nop 0\n\tglobal_load_lds_dwordx4 %1, off nt\n\ts_mov_b32 m0, %0"
               : "=&s"(keep) : "v"(gsrc), "s"(lds_dst) : "memory");
}
#define WAIT_VM(n) asm volatile("s_waitcnt vmcnt(" #n ")" ::: "memory")

DI bf16x8 pack_step(const f32x16& x, int s) {
  u32x4 p;
  asm volatile("s_nop 1\n\tv_cvt_pk_bf16_f32 %0, %4, %5\n\tv_cvt_pk_bf16_f32 %1, %6, %7\n\tv_cvt_pk_bf16_f32 %2, %8, %9\n\tv_cvt_pk_bf16_f32 %3, %10, %11\n\ts_nop 1"
               : "=&v"(p[0]), "=&v"(p[1]), "=&v"(p[2]), "=&v"(p[3])
               : "v"(x[8 * s]), "v"(x[8 * s + 1]), "v"(x[8 * s + 2]), "v"(x[8 * s + 3]), "v"(x[8 * s + 4]), "v"(x[8 * s + 5]), "v"(x[8 * s + 6]), "v"(x[8 * s + 7]));
  return __builtin_bit_cast(bf16x8, p);
}

DI void tr_read4(unsigned a0, unsigned a1, unsigned a2, unsigned a3, bf16x8& f0, bf16x8& f1) {
  s16x4 r0, r1, r2, r3;
  asm volatile("ds_read_b64_tr_b16 %0, %4\n\tds_read_b64_tr_b16 %1, %5\n\tds_read_b64_tr_b16 %2, %6\n\tds_read_b64_tr_b16 %3, %7\n\ts_waitcnt lgkmcnt(0)"
               : "=&v"(r0), "=&v"(r1), "=&v"(r2), "=&v"(r3) : "v"(a0), "v"(a1), "v"(a2), "v"(a3) : "memory");
  f0 = __builtin_shufflevector(r0, r1, 0, 1, 2, 3, 4, 5, 6, 7);
  f1 = __builtin_shufflevector(r2, r3, 0, 1, 2, 3, 4, 5, 6, 7);
}

__global__ __launch_bounds__(256) void k1_qproj(const u16* __restrict__ xA, const u16* __restrict__ wq,
                                                 const u16* __restrict__ xB, const u16* __restrict__ wqB,
                                                 const u16* __restrict__ wuk, u16* __restrict__ qA, u16* __restrict__ qabs) {
  __shared__ float xs[4][512];
  __shared__ __attribute__((aligned(16))) float red[8448];
  __shared__ float qb[4][128];
  const int tid = threadIdx.x, lane = tid & 63, w = tid >> 6;
  const bool partA = blockIdx.x < 64;
  const u16* x = partA ? xA : xB;
  const u32x4 xv4 = *(const u32x4*)(x + tid * 8);
  auto stage_x = [&]() {
    float* d = &xs[0][0] + tid * 8;
#pragma unroll
    for (int j = 0; j < 4; ++j) { d[2 * j] = bfl(xv4[j]); d[2 * j + 1] = bfh(xv4[j]); }
    __syncthreads();
  };
  if (partA) {
    const int col0 = blockIdx.x * 64;
    const int rg = lane >> 3, cg = lane & 7;
    float acc[4][8];
#pragma unroll
    for (int b = 0; b < 4; ++b)
#pragma unroll
      for (int j = 0; j < 8; ++j) acc[b][j] = 0.f;
    u32x4 wv[16];
#pragma unroll
    for (int i = 0; i < 16; ++i) wv[i] = __builtin_nontemporal_load((const u32x4*)(wq + (size_t)(w * 128 + i * 8 + rg) * 4096 + col0 + cg * 8));
    stage_x();
    __builtin_amdgcn_sched_barrier(0);
#pragma unroll
    for (int i = 0; i < 16; ++i) {
      const int row = w * 128 + i * 8 + rg;
      const u32x4 v = wv[i];
#pragma unroll
      for (int b = 0; b < 4; ++b) {
        const float xv = xs[b][row];
#pragma unroll
        for (int j = 0; j < 4; ++j) { acc[b][2 * j] += xv * bfl(v[j]); acc[b][2 * j + 1] += xv * bfh(v[j]); }
      }
    }
    const int grp = w * 8 + rg;
#pragma unroll
    for (int b = 0; b < 4; ++b) {
      *(f32x4*)&red[grp * 260 + b * 64 + cg * 8] = f32x4{acc[b][0], acc[b][1], acc[b][2], acc[b][3]};
      *(f32x4*)&red[grp * 260 + b * 64 + cg * 8 + 4] = f32x4{acc[b][4], acc[b][5], acc[b][6], acc[b][7]};
    }
    __syncthreads();
    {
      float s = 0.f;
#pragma unroll
      for (int g = 0; g < 32; ++g) s += red[g * 260 + tid];
      qA[(tid >> 6) * 4096 + col0 + (tid & 63)] = f2bf(s);
    }
  } else {
    const int id = blockIdx.x - 64, h = id & 31, cs = id >> 5;
    const int rg = tid >> 4, cg = tid & 15;
    u32x4 uk[4];
#pragma unroll
    for (int i = 0; i < 4; ++i) uk[i] = __builtin_nontemporal_load((const u32x4*)(wuk + (size_t)(cs * 64 + i * 16 + rg) * 4096 + h * 128 + cg * 8));
    float acc[4][8];
#pragma unroll
    for (int b = 0; b < 4; ++b)
#pragma unroll
      for (int j = 0; j < 8; ++j) acc[b][j] = 0.f;
    u32x4 wv[32];
#pragma unroll
    for (int i = 0; i < 32; ++i) wv[i] = *(const u32x4*)(wqB + (size_t)(i * 16 + rg) * 4096 + h * 128 + cg * 8);
    stage_x();
    __builtin_amdgcn_sched_barrier(0);
#pragma unroll
    for (int i = 0; i < 32; ++i) {
      const int row = i * 16 + rg;
      const u32x4 v = wv[i];
#pragma unroll
      for (int b = 0; b < 4; ++b) {
        const float xv = xs[b][row];
#pragma unroll
        for (int j = 0; j < 4; ++j) { acc[b][2 * j] += xv * bfl(v[j]); acc[b][2 * j + 1] += xv * bfh(v[j]); }
      }
    }
#pragma unroll
    for (int b = 0; b < 4; ++b) {
      *(f32x4*)&red[rg * 516 + b * 128 + cg * 8] = f32x4{acc[b][0], acc[b][1], acc[b][2], acc[b][3]};
      *(f32x4*)&red[rg * 516 + b * 128 + cg * 8 + 4] = f32x4{acc[b][4], acc[b][5], acc[b][6], acc[b][7]};
    }
    __syncthreads();
#pragma unroll
    for (int k = 0; k < 2; ++k) {
      const int i = tid + k * 256;
      float s = 0.f;
#pragma unroll
      for (int g = 0; g < 16; ++g) s += red[g * 516 + i];
      (&qb[0][0])[i] = rbf(s);
    }
    __syncthreads();
#pragma unroll
    for (int i = 0; i < 4; ++i) {
      const u32x4 v = uk[i];
#pragma unroll
      for (int b = 0; b < 4; ++b) {
        float s = 0.f;
#pragma unroll
        for (int j = 0; j < 4; ++j) { s += qb[b][cg * 8 + 2 * j] * bfl(v[j]); s += qb[b][cg * 8 + 2 * j + 1] * bfh(v[j]); }
        red[(i * 16 + rg) * 68 + b * 17 + cg] = s;
      }
    }
    __syncthreads();
    {
      const int row = tid >> 2, b = tid & 3;
      float s = 0.f;
#pragma unroll
      for (int g = 0; g < 16; ++g) s += red[row * 68 + b * 17 + g];
      qabs[(b * 32 + h) * 512 + cs * 64 + row] = f2bf(s);
    }
  }
}

__global__ __launch_bounds__(256, 2) void k2_attn(const u16* __restrict__ kc, const u16* __restrict__ vc,
                                                const u16* __restrict__ ckv, const u16* __restrict__ qA,
                                                const u16* __restrict__ qabs, float* __restrict__ accB,
                                                float* __restrict__ mlB, float* __restrict__ accA, float* __restrict__ mlA) {
  extern __shared__ __attribute__((aligned(16))) char smem[];
  const int tid = threadIdx.x, lane = tid & 63;
  const int w = __builtin_amdgcn_readfirstlane(tid >> 6);
  const int r = lane & 31, h = lane >> 5;
  const unsigned sbase = (unsigned)(size_t)smem;
  const float LOG2E = 1.4426950408889634f;
  const float scale = 0.08838834764831845f * LOG2E;

  const unsigned blk = (lane >> 4) & 1, q4 = (lane & 15) >> 2, p = lane & 3;
  unsigned trof[4][2][2];
#pragma unroll
  for (int ct = 0; ct < 4; ++ct)
#pragma unroll
    for (int ks = 0; ks < 2; ++ks)
#pragma unroll
      for (int t = 0; t < 2; ++t)
        trof[ct][ks][t] = off_b(16 * ks + 8 * t + 4 * h + q4, 4 * ct + 2 * blk + (p >> 1)) + 8 * (p & 1);
  unsigned rowof[8];
#pragma unroll
  for (int s = 0; s < 8; ++s) rowof[s] = off_b(r, 2 * s + h);

  f32x16 acc[4];
#pragma unroll
  for (int ct = 0; ct < 4; ++ct)
#pragma unroll
    for (int i = 0; i < 16; ++i) acc[ct][i] = 0.f;
  float m_run = -1e30f, l_run = 0.f;
  const unsigned ring = sbase + w * 16384;

  if (blockIdx.x < 256) {
    const int wg = blockIdx.x, b = wg >> 6, sp = wg & 63;
    const int key0 = sp * KEYS_B;
    const u16* src = ckv + ((size_t)b * SK + key0) * DC + w * 128;
    const unsigned sx = sbase + 4 * 16384;
    unsigned goff[8];
#pragma unroll
    for (int i = 0; i < 8; ++i) {
      const unsigned n = lane + 64 * i, row = n >> 4, ch = (n & 15) ^ xr_b(row);
      goff[i] = row * DC + ch * 8;
    }
    auto issue = [&](int t) {
      const u16* tp = src + (size_t)t * 32 * DC;
      const unsigned dst = ring + (t & 1) * 8192;
#pragma unroll
      for (int i = 0; i < 8; ++i) glds16(tp + goff[i], dst + i * 1024);
    };
    issue(0); issue(1);
    bf16x8 qf[8];
#pragma unroll
    for (int s = 0; s < 8; ++s) qf[s] = *(const bf16x8*)(qabs + (b * 32 + r) * 512 + w * 128 + 16 * s + 8 * h);

#pragma unroll
    for (int t = 0; t < 4; ++t) {
      if (t == 0 || t == 3) { WAIT_VM(0); } else { WAIT_VM(8); }
      const unsigned tb = ring + (t & 1) * 8192;
      f32x16 s;
#pragma unroll
      for (int i = 0; i < 16; ++i) s[i] = 0.f;
#pragma unroll
      for (int ks = 0; ks < 8; ++ks) {
        const bf16x8 a = *(const bf16x8*)(smem + (tb - sbase) + rowof[ks]);
        s = MFMA32(a, qf[ks], s);
      }
      if (t > 0) { __builtin_amdgcn_s_barrier(); asm volatile("" ::: "memory"); }
#pragma unroll
      for (int gg = 0; gg < 4; ++gg) {
        f32x4 v = {s[4 * gg], s[4 * gg + 1], s[4 * gg + 2], s[4 * gg + 3]};
        *(f32x4*)(smem + (sx - sbase) + w * 4096 + gg * 1024 + lane * 16) = v;
      }
      asm volatile("s_waitcnt lgkmcnt(0)" ::: "memory");
      __builtin_amdgcn_s_barrier();
      asm volatile("" ::: "memory");
#pragma unroll
      for (int gg = 0; gg < 4; ++gg) {
        f32x4 v0 = *(const f32x4*)(smem + (sx - sbase) + 0 * 4096 + gg * 1024 + lane * 16);
        f32x4 v1 = *(const f32x4*)(smem + (sx - sbase) + 1 * 4096 + gg * 1024 + lane * 16);
        f32x4 v2 = *(const f32x4*)(smem + (sx - sbase) + 2 * 4096 + gg * 1024 + lane * 16);
        f32x4 v3 = *(const f32x4*)(smem + (sx - sbase) + 3 * 4096 + gg * 1024 + lane * 16);
#pragma unroll
        for (int j = 0; j < 4; ++j) s[4 * gg + j] = ((v0[j] + v1[j]) + (v2[j] + v3[j])) * scale;
      }
      float mx = s[0];
#pragma unroll
      for (int i = 1; i < 16; ++i) mx = fmaxf(mx, s[i]);
      mx = fmaxf(mx, __shfl_xor(mx, 32));
      const float m_new = fmaxf(m_run, mx);
      const float alpha = exp2f(m_run - m_new);
      m_run = m_new;
      float ps = 0.f;
#pragma unroll
      for (int i = 0; i < 16; ++i) { s[i] = exp2f(s[i] - m_new); ps += s[i]; }
      l_run = l_run * alpha + ps;
#pragma unroll
      for (int ct = 0; ct < 4; ++ct)
#pragma unroll
        for (int i = 0; i < 16; ++i) acc[ct][i] *= alpha;
      const bf16x8 p0 = pack_step(s, 0), p1 = pack_step(s, 1);
#pragma unroll
      for (int ct = 0; ct < 4; ++ct) {
        bf16x8 a0, a1;
        tr_read4(tb + trof[ct][0][0], tb + trof[ct][0][1], tb + trof[ct][1][0], tb + trof[ct][1][1], a0, a1);
        acc[ct] = MFMA32(a0, p0, acc[ct]);
        acc[ct] = MFMA32(a1, p1, acc[ct]);
      }
      if (t + 2 < 4) issue(t + 2);
    }
    l_run += __shfl_xor(l_run, 32);
#pragma unroll
    for (int ct = 0; ct < 4; ++ct)
#pragma unroll
      for (int gg = 0; gg < 4; ++gg) {
        f32x4 v = {acc[ct][4 * gg], acc[ct][4 * gg + 1], acc[ct][4 * gg + 2], acc[ct][4 * gg + 3]};
        *(f32x4*)(smem + (ring - sbase) + r * 512 + (((ct * 32 + 8 * gg + 4 * h) * 4) ^ ((r & 7) << 4))) = v;
      }
    asm volatile("s_waitcnt lgkmcnt(0)" ::: "memory");
    float* ap = accB + ((size_t)wg * 32) * 512 + w * 128;
#pragma unroll
    for (int i = 0; i < 16; ++i) {
      const int hd = i * 2 + h, c4 = r;
      const f32x4 v = *(const f32x4*)(smem + (ring - sbase) + hd * 512 + ((c4 * 16) ^ ((hd & 7) << 4)));
      *(f32x4*)(ap + (size_t)hd * 512 + c4 * 4) = v;
    }
    if (w == 0 && lane < 32) { mlB[(wg * 32 + r) * 2] = m_run; mlB[(wg * 32 + r) * 2 + 1] = l_run; }
  } else {
    const int wg = blockIdx.x - 256, bk = wg >> 3, oct = wg & 7;
    const int b = bk >> 3, kvh = bk & 7;
    const int split = oct * 4 + w;
    const int key0 = SK - WIN + split * KEYS_A;
    const u16* ksrc = kc + ((size_t)bk * SK + key0) * DH;
    const u16* vsrc = vc + ((size_t)bk * SK + key0) * DH;
    unsigned goff[8];
#pragma unroll
    for (int i = 0; i < 8; ++i) {
      const unsigned n = lane + 64 * i, row = n >> 4, ch = (n & 15) ^ xr_b(row);
      goff[i] = row * DH + ch * 8;
    }
    auto issueK = [&](int t) {
#pragma unroll
      for (int i = 0; i < 8; ++i) glds16(ksrc + (size_t)t * 32 * DH + goff[i], ring + i * 1024);
    };
    auto issueV = [&](int t) {
#pragma unroll
      for (int i = 0; i < 8; ++i) glds16(vsrc + (size_t)t * 32 * DH + goff[i], ring + 8192 + i * 1024);
    };
    bf16x8 qf[8];
#pragma unroll
    for (int s = 0; s < 8; ++s) {
      bf16x8 z = {0, 0, 0, 0, 0, 0, 0, 0};
      qf[s] = z;
      if (r < 4) qf[s] = *(const bf16x8*)(qA + b * 4096 + (kvh * 4 + r) * 128 + 16 * s + 8 * h);
    }
    issueK(0); issueV(0);
#pragma unroll
    for (int t = 0; t < 2; ++t) {
      WAIT_VM(8);
      f32x16 s;
#pragma unroll
      for (int i = 0; i < 16; ++i) s[i] = 0.f;
#pragma unroll
      for (int ks = 0; ks < 8; ++ks) {
        const bf16x8 a = *(const bf16x8*)(smem + (ring - sbase) + rowof[ks]);
        s = MFMA32(a, qf[ks], s);
      }
      asm volatile("s_waitcnt lgkmcnt(0)" ::: "memory");
      if (t == 0) issueK(1);
#pragma unroll
      for (int i = 0; i < 16; ++i) s[i] *= scale;
      float mx = s[0];
#pragma unroll
      for (int i = 1; i < 16; ++i) mx = fmaxf(mx, s[i]);
      mx = fmaxf(mx, __shfl_xor(mx, 32));
      const float m_new = fmaxf(m_run, mx);
      const float alpha = exp2f(m_run - m_new);
      m_run = m_new;
      float ps = 0.f;
#pragma unroll
      for (int i = 0; i < 16; ++i) { s[i] = exp2f(s[i] - m_new); ps += s[i]; }
      l_run = l_run * alpha + ps;
#pragma unroll
      for (int ct = 0; ct < 4; ++ct)
#pragma unroll
        for (int i = 0; i < 16; ++i) acc[ct][i] *= alpha;
      const bf16x8 p0 = pack_step(s, 0), p1 = pack_step(s, 1);
      if (t == 0) { WAIT_VM(8); } else { WAIT_VM(0); }
#pragma unroll
      for (int ct = 0; ct < 4; ++ct) {
        bf16x8 a0, a1;
        tr_read4(ring + 8192 + trof[ct][0][0], ring + 8192 + trof[ct][0][1], ring + 8192 + trof[ct][1][0], ring + 8192 + trof[ct][1][1], a0, a1);
        acc[ct] = MFMA32(a0, p0, acc[ct]);
        acc[ct] = MFMA32(a1, p1, acc[ct]);
      }
      if (t == 0) issueV(1);
    }
    l_run += __shfl_xor(l_run, 32);
    const int pw = bk * NSPLIT_A + split;
    if (r < 4) {
      float* ap = accA + ((size_t)pw * 4 + r) * 128;
#pragma unroll
      for (int ct = 0; ct < 4; ++ct)
#pragma unroll
        for (int gg = 0; gg < 4; ++gg) {
          f32x4 v = {acc[ct][4 * gg], acc[ct][4 * gg + 1], acc[ct][4 * gg + 2], acc[ct][4 * gg + 3]};
          *(f32x4*)(ap + ct * 32 + 8 * gg + 4 * h) = v;
        }
      if (h == 0) { mlA[(pw * 4 + r) * 2] = m_run; mlA[(pw * 4 + r) * 2 + 1] = l_run; }
    }
  }
}

__global__ __launch_bounds__(256) void k3_proj(const float* __restrict__ accB, const float* __restrict__ mlB,
                                                const float* __restrict__ accA, const float* __restrict__ mlA,
                                                const u16* __restrict__ wuv, const u16* __restrict__ woB,
                                                const u16* __restrict__ woA, float* __restrict__ opart) {
  __shared__ float wgt[4][64];
  __shared__ float vec[4][128];
  __shared__ float cls[4][64];
  __shared__ __attribute__((aligned(16))) float red[4][4][512];
  const int tid = threadIdx.x, lane = tid & 63, w = tid >> 6;
  float acc[4][8];
#pragma unroll
  for (int b = 0; b < 4; ++b)
#pragma unroll
    for (int j = 0; j < 8; ++j) acc[b][j] = 0.f;
  if (blockIdx.x < 256) {
    const int h = blockIdx.x & 31, cs = blockIdx.x >> 5;
    float pm, pl;
    {
      const int b = tid >> 6, sp = tid & 63;
      pm = mlB[(((b * 64 + sp) * 32) + h) * 2]; pl = mlB[(((b * 64 + sp) * 32) + h) * 2 + 1];
    }
    float pa[64];
    {
      const int b = tid >> 6, c = cs * 64 + (tid & 63);
#pragma unroll
      for (int sp = 0; sp < 64; ++sp) pa[sp] = __builtin_nontemporal_load(accB + (((size_t)(b * 64 + sp) * 32) + h) * 512 + c);
    }
    const int rg = tid >> 4, cg = tid & 15;
    u32x4 uv[4];
#pragma unroll
    for (int i = 0; i < 4; ++i) uv[i] = __builtin_nontemporal_load((const u32x4*)(wuv + (size_t)(cs * 64 + i * 16 + rg) * 4096 + h * 128 + cg * 8));
    u32x4 wv[32];
    const u16* wo = woB + (size_t)h * 128 * 512;
#pragma unroll
    for (int i = 0; i < 32; ++i) wv[i] = *(const u32x4*)(wo + (size_t)(w * 32 + i) * 512 + lane * 8);
    __builtin_amdgcn_sched_barrier(0);
    {
      const int b = tid >> 6, sp = tid & 63;
      float M = pm;
#pragma unroll
      for (int o = 1; o < 64; o <<= 1) M = fmaxf(M, __shfl_xor(M, o));
      const float e = exp2f(pm - M);
      float L = e * pl;
#pragma unroll
      for (int o = 1; o < 64; o <<= 1) L += __shfl_xor(L, o);
      wgt[b][sp] = e / L;
    }
    __syncthreads();
    {
      const int b = tid >> 6;
      float s = 0.f;
#pragma unroll
      for (int sp = 0; sp < 64; ++sp) s += wgt[b][sp] * pa[sp];
      cls[b][tid & 63] = s;
    }
    __syncthreads();
    {
      float a2[4][8];
#pragma unroll
      for (int b = 0; b < 4; ++b)
#pragma unroll
        for (int j = 0; j < 8; ++j) a2[b][j] = 0.f;
#pragma unroll
      for (int i = 0; i < 4; ++i) {
        const int row = i * 16 + rg;
        const u32x4 v = uv[i];
#pragma unroll
        for (int b = 0; b < 4; ++b) {
          const float xv = cls[b][row];
#pragma unroll
          for (int j = 0; j < 4; ++j) { a2[b][2 * j] += xv * bfl(v[j]); a2[b][2 * j + 1] += xv * bfh(v[j]); }
        }
      }
      float* rf = &red[0][0][0];
#pragma unroll
      for (int b = 0; b < 4; ++b) {
        *(f32x4*)&rf[rg * 512 + b * 128 + cg * 8] = f32x4{a2[b][0], a2[b][1], a2[b][2], a2[b][3]};
        *(f32x4*)&rf[rg * 512 + b * 128 + cg * 8 + 4] = f32x4{a2[b][4], a2[b][5], a2[b][6], a2[b][7]};
      }
    }
    __syncthreads();
#pragma unroll
    for (int k = 0; k < 2; ++k) {
      const int i = tid + k * 256;
      float s = 0.f;
#pragma unroll
      for (int g = 0; g < 16; ++g) s += (&red[0][0][0])[g * 512 + i];
      (&vec[0][0])[i] = s;
    }
    __syncthreads();
#pragma unroll
    for (int i = 0; i < 32; ++i) {
      const int row = w * 32 + i;
      const u32x4 v = wv[i];
#pragma unroll
      for (int b = 0; b < 4; ++b) {
        const float xv = vec[b][row];
#pragma unroll
        for (int j = 0; j < 4; ++j) { acc[b][2 * j] += xv * bfl(v[j]); acc[b][2 * j + 1] += xv * bfh(v[j]); }
      }
    }
  } else {
    const int js = blockIdx.x - 256;
    const int hq = js >> 1, d0 = (js & 1) * 64;
    const int kvh = hq >> 2, hr = hq & 3;
    float pm = 0.f, pl = 0.f;
    if (tid < 128) {
      const int b = tid >> 5, sp = tid & 31;
      const int pw = (b * 8 + kvh) * 32 + sp;
      pm = mlA[(pw * 4 + hr) * 2]; pl = mlA[(pw * 4 + hr) * 2 + 1];
    }
    float pa[32];
    {
      const int b = tid >> 6, d = tid & 63;
#pragma unroll
      for (int sp = 0; sp < 32; ++sp) pa[sp] = accA[((size_t)(((b * 8 + kvh) * 32 + sp) * 4 + hr)) * 128 + d0 + d];
    }
    u32x4 wv[16];
    const u16* wo = woA + (size_t)js * 64 * 512;
#pragma unroll
    for (int i = 0; i < 16; ++i) wv[i] = __builtin_nontemporal_load((const u32x4*)(wo + (size_t)(w * 16 + i) * 512 + lane * 8));
    __builtin_amdgcn_sched_barrier(0);
    if (tid < 128) {
      const int b = tid >> 5, sp = tid & 31;
      float M = pm;
#pragma unroll
      for (int o = 1; o < 32; o <<= 1) M = fmaxf(M, __shfl_xor(M, o));
      const float e = exp2f(pm - M);
      float L = e * pl;
#pragma unroll
      for (int o = 1; o < 32; o <<= 1) L += __shfl_xor(L, o);
      wgt[b][sp] = e / L;
    }
    __syncthreads();
    {
      const int b = tid >> 6, d = tid & 63;
      float s = 0.f;
#pragma unroll
      for (int sp = 0; sp < 32; ++sp) s += wgt[b][sp] * pa[sp];
      vec[b][d] = s;
    }
    __syncthreads();
#pragma unroll
    for (int i = 0; i < 16; ++i) {
      const int row = w * 16 + i;
      const u32x4 v = wv[i];
#pragma unroll
      for (int b = 0; b < 4; ++b) {
        const float xv = vec[b][row];
#pragma unroll
        for (int j = 0; j < 4; ++j) { acc[b][2 * j] += xv * bfl(v[j]); acc[b][2 * j + 1] += xv * bfh(v[j]); }
      }
    }
  }
  __syncthreads();
#pragma unroll
  for (int b = 0; b < 4; ++b)
#pragma unroll
    for (int j = 0; j < 8; ++j) red[w][b][lane * 8 + j] = acc[b][j];
  __syncthreads();
  for (int i = tid; i < 2048; i += 256) {
    const int b = i >> 9, c = i & 511;
    opart[(size_t)blockIdx.x * 2048 + i] = red[0][b][c] + red[1][b][c] + red[2][b][c] + red[3][b][c];
  }
}

__global__ __launch_bounds__(256) void k4_final(const float* __restrict__ opart, u16* __restrict__ out) {
  const int o = blockIdx.x * 16 + (threadIdx.x & 15), part = threadIdx.x >> 4;
  const int b = o >> 10, c = o & 1023;
  __shared__ float red[16][16];
  float s = 0.f;
  if (c < 512) {
    float v[4];
#pragma unroll
    for (int i = 0; i < 4; ++i) v[i] = opart[(size_t)(256 + part * 4 + i) * 2048 + b * 512 + c];
    s = (v[0] + v[1]) + (v[2] + v[3]);
  } else {
    float v[16];
#pragma unroll
    for (int i = 0; i < 16; ++i) v[i] = opart[(size_t)(part * 16 + i) * 2048 + b * 512 + (c - 512)];
#pragma unroll
    for (int i = 0; i < 16; ++i) s += v[i];
  }
  red[part][threadIdx.x & 15] = s;
  __syncthreads();
  if (threadIdx.x < 16) {
    float t = 0.f;
#pragma unroll
    for (int i = 0; i < 16; ++i) t += red[i][threadIdx.x];
    out[o] = f2bf(t);
  }
}

extern "C" void kernel_launch(void* const* d_in, const int* in_sizes, int n_in,
                              void* d_out, int out_size, void* d_ws, size_t ws_size,
                              hipStream_t stream) {
  const u16* xA  = (const u16*)d_in[0];
  const u16* kc  = (const u16*)d_in[1];
  const u16* vc  = (const u16*)d_in[2];
  const u16* wq  = (const u16*)d_in[3];
  const u16* woA = (const u16*)d_in[4];
  const u16* xB  = (const u16*)d_in[5];
  const u16* ckv = (const u16*)d_in[6];
  const u16* wqB = (const u16*)d_in[7];
  const u16* wuk = (const u16*)d_in[8];
  const u16* wuv = (const u16*)d_in[9];
  const u16* woB = (const u16*)d_in[10];
  char* ws = (char*)d_ws;
  u16* qA = (u16*)(ws + OFF_QA);
  u16* qabs = (u16*)(ws + OFF_QABS);
  float* accB = (float*)(ws + OFF_ACCB);
  float* mlB = (float*)(ws + OFF_MLB);
  float* accA = (float*)(ws + OFF_ACCA);
  float* mlA = (float*)(ws + OFF_MLA);
  float* opart = (float*)(ws + OFF_OP);
  const int lds2 = 81920;
  hipFuncSetAttribute((const void*)k2_attn, hipFuncAttributeMaxDynamicSharedMemorySize, lds2);
  k1_qproj<<<320, 256, 0, stream>>>(xA, wq, xB, wqB, wuk, qA, qabs);
  k2_attn<<<512, 256, lds2, stream>>>(kc, vc, ckv, qA, qabs, accB, mlB, accA, mlA);
  k3_proj<<<320, 256, 0, stream>>>(accB, mlB, accA, mlA, wuv, woB, woA, opart);
  k4_final<<<256, 256, 0, stream>>>(opart, (u16*)d_out);
}
```
